```python
import torch

def _softmax(x, axis=-1):
    return torch.softmax(x, dim=axis)
def _gelu(x):
    return torch.nn.functional.gelu(x, approximate="tanh")
def _silu(x):
    return torch.nn.functional.silu(x)
def _sigmoid(x):
    return torch.sigmoid(x)
def _layernorm(x, axis=-1, eps=1e-5):
    m = x.mean(dim=axis, keepdim=True)
    v = x.var(dim=axis, keepdim=True, unbiased=False)
    return (x - m) / torch.sqrt(v + eps)
def _scatter_add(tgt, idx, src, axis=0):
    return tgt.clone().index_add_(axis, idx, src)
def _segment_sum(src, seg, n):
    out = torch.zeros((n,) + src.shape[1:], dtype=src.dtype)
    return out.index_add_(0, seg, src)
def _pad(x, pw):
    flat = tuple(v for pair in reversed(pw) for v in pair)
    return torch.nn.functional.pad(x, flat)

kernel_name = 'interleave_layers__spectral_fnet__swiglu_ffn__prompt'

def setup_inputs():

    _w = (lambda a: (lambda t: t.to(torch.bfloat16) if t.is_floating_point() else t)(torch.as_tensor(a)))
    def _A_setup():
        B, S, D = 1, 2048, 448
        ns = torch.arange(S)[:, None] * torch.arange(S)[None, :]
        nd = torch.arange(D)[:, None] * torch.arange(D)[None, :]
        Ws_re = torch.cos(-2.0 * torch.pi * ns / S).to(float)
        Ws_im = torch.sin(-2.0 * torch.pi * ns / S).to(float)
        Wd_re = torch.cos(-2.0 * torch.pi * nd / D).to(float)
        Wd_im = torch.sin(-2.0 * torch.pi * nd / D).to(float)
        return {
            "x": _w((lambda *s: torch.randn(*s, dtype=torch.bfloat16) * 0.05)(B, S, D)),
            "dft_seq_re": _w(Ws_re),
            "dft_seq_im": _w(Ws_im),
            "dft_hid_re": _w(Wd_re),
            "dft_hid_im": _w(Wd_im),
        }
    _A_in = _A_setup()

    def _B_setup():
        B, S, D, F = 1, 2048, 448, 1792
        return {
            "x": _w((lambda *s: torch.randn(*s, dtype=torch.bfloat16) * 0.05)(B, S, D)),
            "w_gate": _w((lambda *s: torch.randn(*s, dtype=torch.bfloat16) * 0.4)(D, F)),
            "w_up": _w((lambda *s: torch.randn(*s, dtype=torch.bfloat16) * 0.4)(D, F)),
            "w_down": _w((lambda *s: torch.randn(*s, dtype=torch.bfloat16) * 0.4)(F, D)),
        }
    _B_in = _B_setup()

    return {
        "A_x": _A_in["x"],
        "A_dft_seq_re": _A_in["dft_seq_re"],
        "A_dft_seq_im": _A_in["dft_seq_im"],
        "A_dft_hid_re": _A_in["dft_hid_re"],
        "A_dft_hid_im": _A_in["dft_hid_im"],
        "B_x": _B_in["x"],
        "B_w_gate": _B_in["w_gate"],
        "B_w_up": _B_in["w_up"],
        "B_w_down": _B_in["w_down"],
    }

def reference(A_x, A_dft_seq_re, A_dft_seq_im, A_dft_hid_re, A_dft_hid_im, B_x, B_w_gate, B_w_up, B_w_down):
    def _A_ref(x, dft_seq_re, dft_seq_im, dft_hid_re, dft_hid_im):
        a = torch.einsum("bsd,ed->bse", x, dft_hid_re)
        b = torch.einsum("bsd,ed->bse", x, dft_hid_im)
        y = torch.einsum("ts,bse->bte", dft_seq_re, a) - torch.einsum("ts,bse->bte", dft_seq_im, b)
        return y

    def _B_ref(x, w_gate, w_up, w_down):
        g = _silu(x @ w_gate)
        u = x @ w_up
        return (g * u) @ w_down

    x = A_x
    x = x + _A_ref(_layernorm(x, axis=-1), A_dft_seq_re, A_dft_seq_im, A_dft_hid_re, A_dft_hid_im)
    x = x + _B_ref(_layernorm(x, axis=-1), B_w_gate, B_w_up, B_w_down)
    x = x + _A_ref(_layernorm(x, axis=-1), A_dft_seq_re, A_dft_seq_im, A_dft_hid_re, A_dft_hid_im)
    x = x + _B_ref(_layernorm(x, axis=-1), B_w_gate, B_w_up, B_w_down)
    return x
```

```cpp
#include <hip/hip_runtime.h>
#include <hip/hip_bf16.h>
#include <stdint.h>

#define PERSIST 0

namespace fn {
#define GAS __attribute__((address_space(1)))
#define LAS __attribute__((address_space(3)))
#define DEV __device__ __forceinline__
typedef unsigned v4u __attribute__((ext_vector_type(4)));
typedef unsigned v2u __attribute__((ext_vector_type(2)));
typedef float f32x4 __attribute__((ext_vector_type(4)));
typedef __bf16 bf16x8 __attribute__((ext_vector_type(8)));
typedef __bf16 bf16x2 __attribute__((ext_vector_type(2)));
typedef GAS unsigned gu32;
typedef __amdgpu_buffer_rsrc_t rsrc_t;
#define RLX_AGENT __ATOMIC_RELAXED, __HIP_MEMORY_SCOPE_AGENT

constexpr int NTHR = 512;
constexpr unsigned KB = 1024u;
constexpr unsigned CTRL_BYTES = 128 * KB;
constexpr unsigned WD_OFF = CTRL_BYTES;
constexpr unsigned WGU_OFF = WD_OFF + 784 * KB;
constexpr unsigned WDN_OFF = WGU_OFF + 3136 * KB;
constexpr unsigned LAY_OFF = WDN_OFF + 1568 * KB;
constexpr unsigned XN_O = 0;
constexpr unsigned XR_O = XN_O + 1792 * KB;
constexpr unsigned PQ_O = XR_O + 1792 * KB;
constexpr unsigned X1_O = PQ_O + 3584 * KB;
constexpr unsigned ST_O = X1_O + 1792 * KB;
constexpr unsigned HB_O = ST_O + 64 * KB;
constexpr unsigned DP_O = HB_O + 7168 * KB;
constexpr unsigned LAY_BYTES = DP_O + 7168 * KB;
constexpr unsigned WS_BYTES = LAY_OFF + 2 * LAY_BYTES;
constexpr unsigned TILE112 = 112 * KB;
constexpr int C_S = 0;
constexpr int C_H = 128;
constexpr int C_G = 160;
constexpr int C_D = 288;
constexpr int C_X = 320;
constexpr int C_TMO = 448;
constexpr int C_LINES = 449;
constexpr int AUX_SC1 = 16;
constexpr int HAUX = PERSIST ? AUX_SC1 : 0;
constexpr int LDS_BYTES = 160 * 1024;
constexpr int SCR = 144 * 1024;

struct Params {
  const char* x; const char* cre; const char* cim; const char* wdre; const char* wdim;
  const char* wg; const char* wu; const char* wdn; char* out; char* ws;
};

DEV rsrc_t mk(const void* p, unsigned bytes) { return __builtin_amdgcn_make_buffer_rsrc((void*)p, 0, bytes, 0x00020000); }
template <int AUX> DEV v4u ld16(rsrc_t r, int voff, int soff) { return __builtin_amdgcn_raw_buffer_load_b128(r, voff, soff, AUX); }
template <int AUX> DEV void st16(v4u v, rsrc_t r, int voff, int soff) { __builtin_amdgcn_raw_buffer_store_b128(v, r, voff, soff, AUX); }
template <int AUX> DEV void dma16(rsrc_t r, LAS char* dst, int voff, int soff) {
  __builtin_amdgcn_raw_ptr_buffer_load_lds(r, (LAS void*)dst, 16, voff, soff, 0, AUX);
}
DEV f32x4 mfma(v4u a, v4u b, f32x4 c) {
  return __builtin_amdgcn_mfma_f32_16x16x32_bf16(__builtin_bit_cast(bf16x8, a), __builtin_bit_cast(bf16x8, b), c, 0, 0, 0);
}
DEV unsigned pack2(float lo, float hi) { bf16x2 r = {(__bf16)lo, (__bf16)hi}; return __builtin_bit_cast(unsigned, r); }
DEV float blo(unsigned u) { return __builtin_bit_cast(float, u << 16); }
DEV float bhi(unsigned u) { return __builtin_bit_cast(float, u & 0xffff0000u); }
#define WAITV(n) asm volatile("s_waitcnt vmcnt(" #n ")" ::: "memory")
#define WAITL0() asm volatile("s_waitcnt lgkmcnt(0)" ::: "memory")
DEV void bar() { asm volatile("" ::: "memory"); __builtin_amdgcn_s_barrier(); asm volatile("" ::: "memory"); }
DEV void lbar() { WAITL0(); bar(); }
template <int N> DEV void waitv() {
  static_assert(N >= 0 && N <= 63, "");
  asm volatile("s_waitcnt vmcnt(%0)" ::"n"(N) : "memory");
}
DEV v4u pair_chunk(unsigned a0, unsigned a1, unsigned b0, unsigned b1) {
  auto s0 = __builtin_amdgcn_permlane16_swap(a0, b0, false, false);
  auto s1 = __builtin_amdgcn_permlane16_swap(a1, b1, false, false);
  return v4u{s0[0], s1[0], s0[1], s1[1]};
}
DEV float xsum16(float v) { auto s = __builtin_amdgcn_permlane16_swap(__float_as_uint(v), __float_as_uint(v), false, false); return __uint_as_float(s[0]) + __uint_as_float(s[1]); }
DEV float xsum32(float v) { auto s = __builtin_amdgcn_permlane32_swap(__float_as_uint(v), __float_as_uint(v), false, false); return __uint_as_float(s[0]) + __uint_as_float(s[1]); }
DEV float xsum8(float v) { return v + __uint_as_float(__builtin_amdgcn_update_dpp(0u, __float_as_uint(v), 0x128, 0xf, 0xf, true)); }

DEV gu32* ctl(const Params& p, int line) { return (gu32*)p.ws + line * 32; }
DEV void poll_ge(const Params& p, int line, unsigned want) {
  gu32* w = ctl(p, line);
  for (unsigned spins = 0;; ++spins) {
    if (__builtin_amdgcn_readfirstlane(__hip_atomic_load(w, RLX_AGENT)) >= want) break;
    if (spins > (1u << 22)) { __hip_atomic_store(ctl(p, C_TMO), 1u, RLX_AGENT); break; }
  }
  __builtin_amdgcn_fence(__ATOMIC_ACQUIRE, "wavefront");
}
DEV void signal(const Params& p, int line) { __hip_atomic_fetch_add(ctl(p, line), 1u, RLX_AGENT); }

constexpr int S_NSLOT = 18;
constexpr int S_WIN = 32;
template <int ST> struct SWait { static constexpr int issued = ST < 1 ? S_NSLOT : ((S_NSLOT + 2 * (ST - 1)) > 32 ? 32 : (S_NSLOT + 2 * (ST - 1)));
  static constexpr int n0 = 8 * (issued - (2 * ST + 2)); static constexpr int n = n0 > 63 ? 63 : n0; };

DEV void s_issue_stage(rsrc_t rre, rsrc_t rim, LAS char* lds, int seq, int rot, int v0, int v1, int sbase) {
  LAS char* dst = lds + (seq % S_NSLOT) * 8192;
  const int so = sbase + ((seq + 2 * rot) & 31) * 128;
#pragma unroll
  for (int pc = 0; pc < 8; ++pc) {
    const int tg = pc >> 2, kind = (pc >> 1) & 1;
    dma16<0>(kind ? rim : rre, dst + pc * 1024, (pc & 1) ? v1 : v0, so + tg * 65536);
  }
}
DEV void s_map(int b, int& tb, int& j, int& rot) { const int x = b & 7, s = b >> 3; tb = 8 * x + (s >> 2); j = s & 3; rot = (4 * j + (tb & 3)) & 15; }

DEV void s_prefetch(const Params& p, LAS char* lds, int b, int lane) {
  int tb, j, rot; s_map(b, tb, j, rot);
  const rsrc_t rre = mk(p.cre, 2048u * 4096u), rim = mk(p.cim, 2048u * 4096u);
  const int v0 = (lane >> 3) * 4096 + (((lane & 7) ^ (lane >> 4)) << 4);
  const int v1 = (8 + (lane >> 3)) * 4096 + (((lane & 7) ^ (4 + (lane >> 4))) << 4);
#pragma unroll
  for (int s = 0; s < S_NSLOT; ++s) s_issue_stage(rre, rim, lds, s, rot, v0, v1, tb * 32 * 4096);
}
template <int U> DEV void s_loader_step(rsrc_t rre, rsrc_t rim, LAS char* lds, int rot, int v0, int v1, int sbase) {
  waitv<SWait<U>::n>();
  bar();
  if (U >= 1 && 2 * U - 2 + S_NSLOT < 32) {
    s_issue_stage(rre, rim, lds, 2 * U - 2 + S_NSLOT, rot, v0, v1, sbase);
    s_issue_stage(rre, rim, lds, 2 * U - 1 + S_NSLOT, rot, v0, v1, sbase);
  }
  if constexpr (U < 15) s_loader_step<U + 1>(rre, rim, lds, rot, v0, v1, sbase);
}
DEV void phase_S(const Params& p, LAS char* lds, rsrc_t rws, int layer, int b, int w, int lane) {
  int tb, j, rot; s_map(b, tb, j, rot);
  const int lay = LAY_OFF + layer * LAY_BYTES;
  if (w == 7) {
    if (!PERSIST) s_prefetch(p, lds, b, lane);
    if (PERSIST && layer > 0) {
      gu32* base = ctl(p, C_X + (b & 7) * 16);
      for (unsigned spins = 0;; ++spins) {
        const unsigned v = lane < 16 ? __hip_atomic_load(base + lane * 32, RLX_AGENT) : 16u;
        if (__all(v >= 16u)) break;
        if (spins > (1u << 22)) { __hip_atomic_store(ctl(p, C_TMO), 1u, RLX_AGENT); break; }
      }
      __builtin_amdgcn_fence(__ATOMIC_ACQUIRE, "wavefront");
    }
    bar();
    const rsrc_t rre = mk(p.cre, 2048u * 4096u), rim = mk(p.cim, 2048u * 4096u);
    const int v0 = (lane >> 3) * 4096 + (((lane & 7) ^ (lane >> 4)) << 4);
    const int v1 = (8 + (lane >> 3)) * 4096 + (((lane & 7) ^ (4 + (lane >> 4))) << 4);
    s_loader_step<0>(rre, rim, lds, rot, v0, v1, tb * 32 * 4096);
    bar();
    if (PERSIST && lane == 0) signal(p, C_S + layer * 64 + tb);
  } else {
    bar();
    const int df = 7 * j + w;
    const int abase = lay + XN_O + df * 64 * 1024;
    v4u a[S_WIN];
#pragma unroll
    for (int i = 0; i < S_WIN; ++i) a[i] = ld16<HAUX>(rws, lane * 16, abase + ((i + 4 * rot) & 63) * 1024);
    __builtin_amdgcn_sched_barrier(0);
    f32x4 acc[4] = {};
    const int t = lane & 15, kg = lane >> 4;
    LAS char* b0 = lds + t * 128 + ((kg ^ (t >> 1)) << 4);
    LAS char* b1 = lds + t * 128 + (((4 + kg) ^ (t >> 1)) << 4);
#pragma unroll
    for (int ks = 0; ks < 64; ++ks) {
      if ((ks & 3) == 0) lbar();
      const v4u cur = a[ks % S_WIN];
      if (ks + S_WIN < 64) a[ks % S_WIN] = ld16<HAUX>(rws, lane * 16, abase + ((ks + S_WIN + 4 * rot) & 63) * 1024);
      LAS char* bs = ((ks & 1) ? b1 : b0) + ((ks >> 1) % S_NSLOT) * 8192;
#pragma unroll
      for (int tf = 0; tf < 4; ++tf) acc[tf] = mfma(cur, *(LAS v4u*)(bs + tf * 2048), acc[tf]);
    }
    const int row = lane >> 4, kind = row & 1, half = row >> 1;
#pragma unroll
    for (int tg = 0; tg < 2; ++tg) {
      const f32x4 r = acc[2 * tg], m = acc[2 * tg + 1];
      const v4u c = pair_chunk(pack2(r[0], r[1]), pack2(r[2], r[3]), pack2(m[0], m[1]), pack2(m[2], m[3]));
      const int tt = 2 * tb + tg;
      st16<HAUX>(c, rws, kind * 14 * 1024 + ((2 * (df & 1) + half) * 16 + t) * 16, lay + PQ_O + (tt * 28 + (df >> 1)) * 1024);
    }
    WAITV(0);
    bar();
  }
}

DEV void h_weights(rsrc_t rws, int b, int w, int lane, v4u (&a)[28]) {
  const int j = (b >> 3) & 3;
  const int base = WD_OFF + (7 * j + w) * 28 * 1024;
#pragma unroll
  for (int i = 0; i < 28; ++i) a[i] = ld16<0>(rws, lane * 16, base + i * 1024);
  __builtin_amdgcn_sched_barrier(0);
}
DEV void phase_H(const Params& p, LAS char* lds, rsrc_t rws, int layer, int b, int w, int lane, v4u (&a)[28]) {
  const int x = b & 7, s = b >> 3, tb = 8 * x + (s >> 2), j = s & 3;
  const int lay = LAY_OFF + layer * LAY_BYTES;
  LAS float* red = (LAS float*)(lds + SCR);
  if (w == 7) {
    if (PERSIST) poll_ge(p, C_S + layer * 64 + tb, 4u);
    const int src = lay + PQ_O + 2 * tb * 28 * 1024;
#pragma unroll
    for (int ks = 0; ks < 28; ++ks) {
      dma16<HAUX>(rws, lds + (ks * 2) * 1024, lane * 16, src + ks * 1024);
      dma16<HAUX>(rws, lds + (ks * 2 + 1) * 1024, lane * 16, src + (28 + ks) * 1024);
    }
    waitv<48>(); bar();
    waitv<32>(); bar();
    waitv<0>(); bar();
    bar();
    if (lane < 32) {
      float mean = 0.f;
#pragma unroll
      for (int ww = 0; ww < 7; ++ww) mean += red[(ww * 32 + lane) * 2];
      mean *= (1.f / 7.f);
      float m2 = 0.f;
#pragma unroll
      for (int ww = 0; ww < 7; ++ww) { const float d = red[(ww * 32 + lane) * 2] - mean; m2 += red[(ww * 32 + lane) * 2 + 1] + 16.f * d * d; }
      const int c = tb >> 2, tok = (tb & 3) * 32 + lane;
      __builtin_amdgcn_raw_buffer_store_b64(v2u{__float_as_uint(mean), __float_as_uint(m2)}, rws, ((c * 4 + j) * 128 + tok) * 8, lay + ST_O, HAUX);
    }
    WAITV(0);
    bar();
    if (PERSIST && lane == 0) signal(p, C_H + layer * 16 + (tb >> 2));
  } else {
    const int ef = 7 * j + w;
    const int row = lane >> 4, t2 = row & 1, half = row >> 1, tok = lane & 15;
    const int kg = 2 * (ef & 1) + half;
    f32x4 acc[2] = {};
    LAS char* bb = lds + lane * 16;
    v4u res;
#pragma unroll
    for (int ks = 0; ks < 28; ++ks) {
      if (ks == 0) { bar(); res = ld16<HAUX>(rws, t2 * 14 * 1024 + (kg * 16 + tok) * 16, lay + XR_O + (2 * tb * 14 + (ef >> 1)) * 1024); }
      if (ks == 4 || ks == 12) lbar();
      acc[0] = mfma(a[ks], *(LAS v4u*)(bb + (ks * 2) * 1024), acc[0]);
      acc[1] = mfma(a[ks], *(LAS v4u*)(bb + (ks * 2 + 1) * 1024), acc[1]);
    }
    float y[8];
#pragma unroll
    for (int i = 0; i < 4; ++i) {
      auto sw = __builtin_amdgcn_permlane16_swap(__float_as_uint(acc[0][i]), __float_as_uint(acc[1][i]), false, false);
      y[i] = __uint_as_float(sw[0]); y[4 + i] = __uint_as_float(sw[1]);
    }
    v4u o; float v[8];
#pragma unroll
    for (int i = 0; i < 4; ++i) {
      o[i] = pack2(blo(res[i]) + y[2 * i], bhi(res[i]) + y[2 * i + 1]);
      v[2 * i] = blo(o[i]); v[2 * i + 1] = bhi(o[i]);
    }
    const int tt = 2 * tb + t2, c = tt >> 3, tp = (tt >> 1) & 3;
    st16<HAUX>(o, rws, (c * 4 + tp) * 28 * 1024 + t2 * 1024 + (kg * 16 + tok) * 16, lay + X1_O + (ef >> 1) * 2048);
    float sm = ((v[0] + v[1]) + (v[2] + v[3])) + ((v[4] + v[5]) + (v[6] + v[7]));
    const float mean = xsum32(sm) * (1.f / 16.f);
    float m2 = 0.f;
#pragma unroll
    for (int i = 0; i < 8; ++i) { const float d = v[i] - mean; m2 += d * d; }
    m2 = xsum32(m2);
    if (half == 0) { red[(w * 32 + t2 * 16 + tok) * 2] = mean; red[(w * 32 + t2 * 16 + tok) * 2 + 1] = m2; }
    lbar();
    WAITV(0);
    bar();
  }
}

DEV void g_map(int b, int& c, int& ft) { const int x = b & 7, s = b >> 3; c = 8 * (x & 1) + (s >> 2); ft = 4 * (x >> 1) + (s & 3); }
DEV void g_weights(rsrc_t rws, int b, int w, int lane, v4u (&a)[28]) {
  int c, ft; g_map(b, c, ft);
  const int base = WGU_OFF + (ft * 7 + w) * 28 * 1024;
#pragma unroll
  for (int i = 0; i < 28; ++i) a[i] = ld16<0>(rws, lane * 16, base + i * 1024);
  __builtin_amdgcn_sched_barrier(0);
}
DEV void load_tile112(rsrc_t rws, LAS char* lds, int src, int lane) {
#pragma unroll
  for (int i = 0; i < 56; ++i) dma16<HAUX>(rws, lds + i * 1024, lane * 16, src + i * 1024);
  waitv<28>(); bar();
#pragma unroll
  for (int i = 56; i < 112; ++i) dma16<HAUX>(rws, lds + i * 1024, lane * 16, src + i * 1024);
}
DEV void phase_G(const Params& p, LAS char* lds, rsrc_t rws, int layer, int b, int w, int lane, v4u (&a)[28]) {
  int c, ft; g_map(b, c, ft);
  const int lay = LAY_OFF + layer * LAY_BYTES;
  LAS float* mr = (LAS float*)(lds + SCR);
  if (w == 7) {
    if (PERSIST) poll_ge(p, C_H + layer * 16 + c, 16u);
    v2u st[2][4];
#pragma unroll
    for (int hh = 0; hh < 2; ++hh)
#pragma unroll
      for (int j = 0; j < 4; ++j) st[hh][j] = __builtin_amdgcn_raw_buffer_load_b64(rws, ((c * 4 + j) * 128 + hh * 64 + lane) * 8, lay + ST_O, HAUX);
#pragma unroll
    for (int hh = 0; hh < 2; ++hh) {
      float mean = 0.f;
#pragma unroll
      for (int j = 0; j < 4; ++j) mean += __uint_as_float(st[hh][j].x);
      mean *= 0.25f;
      float m2 = 0.f;
#pragma unroll
      for (int j = 0; j < 4; ++j) { const float d = __uint_as_float(st[hh][j].x) - mean; m2 += __uint_as_float(st[hh][j].y) + 112.f * d * d; }
      mr[(hh * 64 + lane) * 2] = mean;
      mr[(hh * 64 + lane) * 2 + 1] = __builtin_amdgcn_rsqf(m2 * (1.f / 448.f) + 1e-5f);
    }
    WAITL0();
    load_tile112(rws, lds, lay + X1_O + c * TILE112, lane);
    waitv<56>(); bar();
    waitv<0>(); bar();
    bar();
    if (PERSIST && lane == 0) signal(p, C_G + (layer * 16 + c) * 4 + (ft >> 2));
  } else {
    f32x4 cg = {}, cu = {};
    const v4u ones = {0x3f803f80u, 0x3f803f80u, 0x3f803f80u, 0x3f803f80u};
#pragma unroll
    for (int ks = 0; ks < 14; ++ks) { cg = mfma(a[ks], ones, cg); cu = mfma(a[14 + ks], ones, cu); }
    const int row = lane >> 4, t2l = row & 1, half = row >> 1, tok = lane & 15;
    const int ff = 7 * ft + w, ksg = ff >> 1, q = ksg / 14, ks14 = ksg - 14 * q;
    const int hbase = lay + HB_O + (c * 4 + q) * TILE112 + ks14 * 2048;
    const int hv = t2l * 1024 + ((2 * (ff & 1) + half) * 16 + tok) * 16;
    LAS char* bb = lds + lane * 16;
    f32x4 ag[2][2], au[2][2];
    auto mm = [&](int tp, f32x4(&g)[2], f32x4(&u)[2]) {
      g[0] = g[1] = u[0] = u[1] = f32x4{0.f, 0.f, 0.f, 0.f};
#pragma unroll
      for (int ks = 0; ks < 14; ++ks) {
        const v4u b0 = *(LAS v4u*)(bb + (tp * 28 + ks * 2) * 1024), b1 = *(LAS v4u*)(bb + (tp * 28 + ks * 2 + 1) * 1024);
        g[0] = mfma(a[ks], b0, g[0]); u[0] = mfma(a[14 + ks], b0, u[0]);
        g[1] = mfma(a[ks], b1, g[1]); u[1] = mfma(a[14 + ks], b1, u[1]);
      }
    };
    auto epi = [&](int tp, f32x4(&g)[2], f32x4(&u)[2]) {
      unsigned pk[2][2];
#pragma unroll
      for (int t2 = 0; t2 < 2; ++t2) {
        const float mean = mr[((2 * tp + t2) * 16 + tok) * 2], rs = mr[((2 * tp + t2) * 16 + tok) * 2 + 1];
        float h[4];
#pragma unroll
        for (int i = 0; i < 4; ++i) {
          const float gg = rs * (g[t2][i] - mean * cg[i]), uu = rs * (u[t2][i] - mean * cu[i]);
          h[i] = gg * uu * __builtin_amdgcn_rcpf(1.f + __expf(-gg));
        }
        pk[t2][0] = pack2(h[0], h[1]); pk[t2][1] = pack2(h[2], h[3]);
      }
      st16<HAUX>(pair_chunk(pk[0][0], pk[0][1], pk[1][0], pk[1][1]), rws, hv, hbase + tp * 28 * 1024);
    };
    bar();
    mm(0, ag[0], au[0]);
    lbar();
    mm(1, ag[1], au[1]); epi(0, ag[0], au[0]);
    lbar();
    mm(2, ag[0], au[0]); epi(1, ag[1], au[1]);
    mm(3, ag[1], au[1]); epi(2, ag[0], au[0]);
    epi(3, ag[1], au[1]);
    WAITV(0);
    bar();
  }
}

DEV void d_map(int b, int& c, int& j, int& q) { const int x = b & 7, s = b >> 3; c = 8 * (x & 1) + (s >> 2); j = s & 3; q = x >> 1; }
DEV void d_weights(rsrc_t rws, int b, int w, int lane, v4u (&a)[28]) {
  int c, j, q; d_map(b, c, j, q);
  const int base = WDN_OFF + ((q * 4 + j) * 7 + w) * 14 * 1024;
#pragma unroll
  for (int i = 0; i < 14; ++i) a[i] = ld16<0>(rws, lane * 16, base + i * 1024);
  __builtin_amdgcn_sched_barrier(0);
}
DEV void phase_D(const Params& p, LAS char* lds, rsrc_t rws, int layer, int b, int w, int lane, v4u (&a)[28]) {
  int c, j, q; d_map(b, c, j, q);
  const int lay = LAY_OFF + layer * LAY_BYTES;
  if (w == 7) {
    if (PERSIST) poll_ge(p, C_G + (layer * 16 + c) * 4 + q, 4u);
    load_tile112(rws, lds, lay + HB_O + (c * 4 + q) * TILE112, lane);
    waitv<56>(); bar();
    waitv<0>(); bar();
    bar();
    if (PERSIST && lane == 0) signal(p, C_D + layer * 16 + c);
  } else {
    const int row = lane >> 4, t2l = row & 1, half = row >> 1, tok = lane & 15;
    const int df = 7 * j + w;
    const int dbase = lay + DP_O + (q * 16 + c) * TILE112 + (df >> 1) * 2048;
    const int dv = t2l * 1024 + ((2 * (df & 1) + half) * 16 + tok) * 16;
    LAS char* bb = lds + lane * 16;
#pragma unroll
    for (int tp = 0; tp < 4; ++tp) {
      if (tp == 0) bar();
      if (tp == 1 || tp == 2) lbar();
      f32x4 y0 = {}, y1 = {};
#pragma unroll
      for (int ks = 0; ks < 14; ++ks) {
        y0 = mfma(a[ks], *(LAS v4u*)(bb + (tp * 28 + ks * 2) * 1024), y0);
        y1 = mfma(a[ks], *(LAS v4u*)(bb + (tp * 28 + ks * 2 + 1) * 1024), y1);
      }
      st16<HAUX>(pair_chunk(pack2(y0[0], y0[1]), pack2(y0[2], y0[3]), pack2(y1[0], y1[1]), pack2(y1[2], y1[3])), rws, dv, dbase + tp * 28 * 1024);
    }
    WAITV(0);
    bar();
  }
}

DEV void ln_publish(rsrc_t rws, LAS char* lds, int laynext, int tok0  , int tid, const float (&v)[8]) {
  LAS float* red = (LAS float*)(lds + SCR);
  LAS char* tr = lds + SCR + 1024;
  const int wv = tid >> 6, tok = tid & 7, c56 = tid >> 3;
  float mean = 0.f, rs = 0.f;
  if (tid < 448) {
    float sm = ((v[0] + v[1]) + (v[2] + v[3])) + ((v[4] + v[5]) + (v[6] + v[7]));
    sm = xsum32(xsum16(xsum8(sm)));
    const float mw = sm * (1.f / 64.f);
    float m2 = 0.f;
#pragma unroll
    for (int i = 0; i < 8; ++i) { const float d = v[i] - mw; m2 += d * d; }
    m2 = xsum32(xsum16(xsum8(m2)));
    if ((tid & 63) < 8) { red[(wv * 8 + tok) * 2] = mw; red[(wv * 8 + tok) * 2 + 1] = m2; }
  }
  lbar();
  if (tid < 448) {
#pragma unroll
    for (int ww = 0; ww < 7; ++ww) mean += red[(ww * 8 + tok) * 2];
    mean *= (1.f / 7.f);
    float m2 = 0.f;
#pragma unroll
    for (int ww = 0; ww < 7; ++ww) { const float d = red[(ww * 8 + tok) * 2] - mean; m2 += red[(ww * 8 + tok) * 2 + 1] + 64.f * d * d; }
    rs = __builtin_amdgcn_rsqf(m2 * (1.f / 448.f) + 1e-5f);
#pragma unroll
    for (int i = 0; i < 8; ++i) {
      const __bf16 hv = (__bf16)((v[i] - mean) * rs);
      *(LAS unsigned short*)(tr + (c56 * 8 + i) * 16 + c56 * 16 + tok * 2) = __builtin_bit_cast(unsigned short, hv);
    }
  }
  lbar();
  if (tid < 448) {
    const int d = tid;
    const v4u o = *(LAS v4u*)(tr + d * 16 + (d >> 3) * 16);
    st16<HAUX>(o, rws, ((d >> 4) * 64) * 1024 + (d & 15) * 16, laynext + XN_O + (tok0 >> 5) * 1024 + ((tok0 >> 3) & 3) * 256);
  }
}
DEV void phase_X(const Params& p, LAS char* lds, rsrc_t rws, int layer, int b, int w, int lane) {
  const int x = b & 7, s = b >> 3, c = 2 * x + (s >> 4), r = s & 15;
  const int lay = LAY_OFF + layer * LAY_BYTES;
  const int tid = w * 64 + lane;
  if (PERSIST) { if (w == 7) poll_ge(p, C_D + layer * 16 + c, 16u); bar(); }
  float v[8];
  const int tok = tid & 7, c56 = tid >> 3, ks = c56 >> 2, kg = c56 & 3;
  if (tid < 448) {
    const int tp = r >> 2, t2 = (r >> 1) & 1, tok16 = 8 * (r & 1) + tok;
    const int off = (tp * 14 + ks) * 2048 + t2 * 1024 + (kg * 16 + tok16) * 16;
    v4u pr[4];
#pragma unroll
    for (int q = 0; q < 4; ++q) pr[q] = ld16<HAUX>(rws, off, lay + DP_O + (q * 16 + c) * TILE112);
    const v4u x1 = ld16<HAUX>(rws, off, lay + X1_O + c * TILE112);
    v4u o;
#pragma unroll
    for (int i = 0; i < 4; ++i) {
      const float lo = blo(x1[i]) + ((blo(pr[0][i]) + blo(pr[1][i])) + (blo(pr[2][i]) + blo(pr[3][i])));
      const float hi = bhi(x1[i]) + ((bhi(pr[0][i]) + bhi(pr[1][i])) + (bhi(pr[2][i]) + bhi(pr[3][i])));
      o[i] = pack2(lo, hi); v[2 * i] = blo(o[i]); v[2 * i + 1] = bhi(o[i]);
    }
    if (layer == 1) st16<0>(o, mk(p.out, 2048u * 896u), (128 * c + 8 * r + tok) * 896 + c56 * 16, 0);
    else st16<HAUX>(o, rws, ((8 * c + (r >> 1)) * 14 + ks) * 1024 + (kg * 16 + tok16) * 16, lay + LAY_BYTES + XR_O);
  }
  if (layer == 0) {
    ln_publish(rws, lds, lay + LAY_BYTES, 128 * c + 8 * r, tid, v);
    WAITV(0);
    bar();
    if (PERSIST && tid >= 448 && tid < 456) signal(p, C_X + (tid - 448) * 16 + c);
  }
}

__global__ void __launch_bounds__(NTHR, 2) k_prep(Params p) {
  extern __shared__ __attribute__((aligned(16))) char smem[];
  LAS char* lds = (LAS char*)smem;
  const rsrc_t rws = mk(p.ws, WS_BYTES);
  const int tid = threadIdx.x, lane = tid & 63, w = __builtin_amdgcn_readfirstlane(tid >> 6), b = blockIdx.x;
  if (b == 0) for (int i = tid; i < C_LINES; i += NTHR) ((gu32*)p.ws)[i * 32] = 0u;
  float v[8];
  {
    const int tok = tid & 7, c56 = tid >> 3;
    if (tid < 448) {
      const v4u xv = ld16<0>(mk(p.x, 2048u * 896u), (8 * b + tok) * 896 + c56 * 16, 0);
#pragma unroll
      for (int i = 0; i < 4; ++i) { v[2 * i] = blo(xv[i]); v[2 * i + 1] = bhi(xv[i]); }
      const int tt = b >> 1, tok16 = 8 * (b & 1) + tok;
      st16<0>(xv, rws, (tt * 14 + (c56 >> 2)) * 1024 + ((c56 & 3) * 16 + tok16) * 16, LAY_OFF + XR_O);
    }
  }
  {
    const int blk = b * 8 + w;
    if (blk < 784) {
      const int ef = blk / 28, ks = blk - ef * 28;
      const bool im = ks >= 14;
      v4u s = ld16<0>(mk(im ? p.wdim : p.wdre, 448u * 896u), (16 * ef + (lane & 15)) * 896 + (im ? ks - 14 : ks) * 64 + (lane >> 4) * 16, 0);
      if (im) s ^= 0x80008000u;
      st16<0>(s, rws, lane * 16, WD_OFF + blk * 1024);
    }
  }
  if (w < 3) {
    const int job = b * 3 + w;
    if (job < 672) {
      const char* src; unsigned bytes; int ld, goff, dst, dstr;
      if (job < 448) { const int ft = job / 28, rem = job - ft * 28, gu = rem / 14, ks = rem - gu * 14;
        src = gu ? p.wu : p.wg; bytes = 448u * 3584u; ld = 3584; goff = ks * 32 * ld + ft * 224;
        dst = WGU_OFF + ((ft * 7 * 2 + gu) * 14 + ks) * 1024; dstr = 28 * 1024;
      } else { const int jj = job - 448, j = jj / 56, ksg = jj - j * 56, q = ksg / 14, ks = ksg - q * 14;
        src = p.wdn; bytes = 1792u * 896u; ld = 896; goff = ksg * 32 * ld + j * 224;
        dst = WDN_OFF + (((q * 4 + j) * 7) * 14 + ks) * 1024; dstr = 14 * 1024; }
      LAS char* t = lds + w * 8192;
      const rsrc_t rsrc = mk(src, bytes);
#pragma unroll
      for (int i = 0; i < 7; ++i) { const int ch = i * 64 + lane, r = ch / 14, cc = ch - r * 14;
        *(LAS v4u*)(t + r * 240 + cc * 16) = ld16<0>(rsrc, goff + r * ld + cc * 16, 0); }
      WAITL0();
      const int n = lane & 15, kg = lane >> 4;
#pragma unroll
      for (int f = 0; f < 7; ++f) {
        unsigned e[8];
#pragma unroll
        for (int i = 0; i < 8; ++i) e[i] = *(LAS unsigned short*)(t + (8 * kg + i) * 240 + (16 * f + n) * 2);
        st16<0>(v4u{e[0] | (e[1] << 16), e[2] | (e[3] << 16), e[4] | (e[5] << 16), e[6] | (e[7] << 16)}, rws, lane * 16, dst + f * dstr);
      }
    }
  }
  ln_publish(rws, lds, LAY_OFF, 8 * b, tid, v);
}

template <int PH> __global__ void __launch_bounds__(NTHR, 2) k_phase(Params p, int layer, int pad) {
  extern __shared__ __attribute__((aligned(16))) char smem[];
  LAS char* lds = (LAS char*)smem;
  const rsrc_t rws = mk(p.ws, WS_BYTES);
  const int lane = threadIdx.x & 63, w = __builtin_amdgcn_readfirstlane(threadIdx.x >> 6), b = blockIdx.x;
  v4u a[28];
  if (PH == 0) phase_S(p, lds, rws, layer, b, w, lane);
  if (PH == 1) { if (w < 7) h_weights(rws, b, w, lane, a); phase_H(p, lds, rws, layer, b, w, lane, a); }
  if (PH == 2) { if (w < 7) g_weights(rws, b, w, lane, a); phase_G(p, lds, rws, layer, b, w, lane, a); }
  if (PH == 3) { if (w < 7) d_weights(rws, b, w, lane, a); phase_D(p, lds, rws, layer, b, w, lane, a); }
  if (PH == 4) phase_X(p, lds, rws, layer, b, w, lane);
}
}

extern "C" void kernel_launch(void* const* d_in, const int* in_sizes, int n_in, void* d_out, int out_size, void* d_ws, size_t ws_size,
                              hipStream_t stream) {
  using namespace fn;
  Params p;
  p.x = (const char*)d_in[0]; p.cre = (const char*)d_in[1]; p.cim = (const char*)d_in[2]; p.wdre = (const char*)d_in[3]; p.wdim = (const char*)d_in[4];
  p.wg = (const char*)d_in[6]; p.wu = (const char*)d_in[7]; p.wdn = (const char*)d_in[8]; p.out = (char*)d_out; p.ws = (char*)d_ws;
  (void)hipFuncSetAttribute((const void*)k_prep, hipFuncAttributeMaxDynamicSharedMemorySize, LDS_BYTES);
  k_prep<<<256, NTHR, LDS_BYTES, stream>>>(p);
  (void)hipFuncSetAttribute((const void*)k_phase<0>, hipFuncAttributeMaxDynamicSharedMemorySize, LDS_BYTES);
  (void)hipFuncSetAttribute((const void*)k_phase<1>, hipFuncAttributeMaxDynamicSharedMemorySize, LDS_BYTES);
  (void)hipFuncSetAttribute((const void*)k_phase<2>, hipFuncAttributeMaxDynamicSharedMemorySize, LDS_BYTES);
  (void)hipFuncSetAttribute((const void*)k_phase<3>, hipFuncAttributeMaxDynamicSharedMemorySize, LDS_BYTES);
  (void)hipFuncSetAttribute((const void*)k_phase<4>, hipFuncAttributeMaxDynamicSharedMemorySize, LDS_BYTES);
  for (int l = 0; l < 2; ++l) {
    k_phase<0><<<256, NTHR, LDS_BYTES, stream>>>(p, l, 0);
    k_phase<1><<<256, NTHR, LDS_BYTES, stream>>>(p, l, 0);
    k_phase<2><<<256, NTHR, LDS_BYTES, stream>>>(p, l, 0);
    k_phase<3><<<256, NTHR, LDS_BYTES, stream>>>(p, l, 0);
    k_phase<4><<<256, NTHR, LDS_BYTES, stream>>>(p, l, 0);
  }
}
```

```cpp
#include <hip/hip_runtime.h>
#include <hip/hip_bf16.h>
#include <stdint.h>
#include <utility>

namespace fn {
#define GAS __attribute__((address_space(1)))
#define LAS __attribute__((address_space(3)))
#define DEV __device__ __forceinline__
typedef unsigned v4u __attribute__((ext_vector_type(4)));
typedef unsigned v2u __attribute__((ext_vector_type(2)));
typedef float f32x4 __attribute__((ext_vector_type(4)));
typedef __bf16 bf16x8 __attribute__((ext_vector_type(8)));
typedef __bf16 bf16x2 __attribute__((ext_vector_type(2)));
typedef GAS unsigned gu32;
typedef __amdgpu_buffer_rsrc_t rsrc_t;
#define RLX_AGENT __ATOMIC_RELAXED, __HIP_MEMORY_SCOPE_AGENT

constexpr int NTHR = 512;
constexpr unsigned KB = 1024u;
constexpr unsigned CTRL_BYTES = 128 * KB;
constexpr unsigned WD_OFF = CTRL_BYTES;
constexpr unsigned WGU_OFF = WD_OFF + 784 * KB;
constexpr unsigned WDN_OFF = WGU_OFF + 3136 * KB;
constexpr unsigned CSP_OFF = WDN_OFF + 1568 * KB;
constexpr unsigned LAY_OFF = CSP_OFF + 256 * KB;
constexpr unsigned XN_O = 0;
constexpr unsigned XR_O = XN_O + 1792 * KB;
constexpr unsigned PQ_O = XR_O + 1792 * KB;
constexpr unsigned X1_O = PQ_O + 3584 * KB;
constexpr unsigned ST_O = X1_O + 1792 * KB;
constexpr unsigned HB_O = ST_O + 64 * KB;
constexpr unsigned DP_O = HB_O + 7168 * KB;
constexpr unsigned LAY_BYTES = DP_O + 7168 * KB;
constexpr unsigned WS_BYTES = LAY_OFF + 2 * LAY_BYTES;
constexpr unsigned TILE112 = 112 * KB;
constexpr int C_S = 0;
constexpr int C_H = 128;
constexpr int C_G = 160;
constexpr int C_D = 288;
constexpr int C_X = 320;
constexpr int C_W = 448;
constexpr int C_TMO = 576;
constexpr int C_LINES = 577;
constexpr unsigned EPOCH_OFF = 80 * KB;
constexpr unsigned TAG_OFF = 96 * KB;
constexpr int AUX_SC1 = 16;
constexpr int HAUX = AUX_SC1;
constexpr unsigned SPIN_CAP = 1u << 16;
constexpr int LDS_BYTES = 160 * 1024;
constexpr int SCR = 144 * 1024;

struct Params {
  const char* x; const char* cre; const char* cim; const char* wdre; const char* wdim;
  const char* wg; const char* wu; const char* wdn; char* out; char* ws;
};

DEV rsrc_t mk(const void* p, unsigned bytes) { return __builtin_amdgcn_make_buffer_rsrc((void*)p, 0, bytes, 0x00020000); }
template <int AUX> DEV v4u ld16(rsrc_t r, int voff, int soff) { return __builtin_amdgcn_raw_buffer_load_b128(r, voff, soff, AUX); }
template <int AUX> DEV void st16(v4u v, rsrc_t r, int voff, int soff) { __builtin_amdgcn_raw_buffer_store_b128(v, r, voff + soff, 0, AUX); }
template <int AUX> DEV void dma16(rsrc_t r, LAS char* dst, int voff, int soff) {
  __builtin_amdgcn_raw_ptr_buffer_load_lds(r, (LAS void*)dst, 16, voff, soff, 0, AUX);
}
DEV f32x4 mfma(v4u a, v4u b, f32x4 c) {
  return __builtin_amdgcn_mfma_f32_16x16x32_bf16(__builtin_bit_cast(bf16x8, a), __builtin_bit_cast(bf16x8, b), c, 0, 0, 0);
}
DEV unsigned pack2(float lo, float hi) { bf16x2 r = {(__bf16)lo, (__bf16)hi}; return __builtin_bit_cast(unsigned, r); }
DEV float blo(unsigned u) { return __builtin_bit_cast(float, u << 16); }
DEV float bhi(unsigned u) { return __builtin_bit_cast(float, u & 0xffff0000u); }
#define WAITV(n) asm volatile("s_waitcnt vmcnt(" #n ")" ::: "memory")
#define WAITL0() asm volatile("s_waitcnt lgkmcnt(0)" ::: "memory")
DEV void bar() { asm volatile("" ::: "memory"); __builtin_amdgcn_s_barrier(); asm volatile("" ::: "memory"); }
DEV void lbar() { WAITL0(); bar(); }
template <int N> DEV void waitv() {
  static_assert(N >= 0 && N <= 63, "");
  asm volatile("s_waitcnt vmcnt(%0)" ::"n"(N) : "memory");
}
DEV v4u pair_chunk(unsigned a0, unsigned a1, unsigned b0, unsigned b1) {
  auto s0 = __builtin_amdgcn_permlane16_swap(a0, b0, false, false);
  auto s1 = __builtin_amdgcn_permlane16_swap(a1, b1, false, false);
  return v4u{s0[0], s1[0], s0[1], s1[1]};
}
DEV float xsum16(float v) { auto s = __builtin_amdgcn_permlane16_swap(__float_as_uint(v), __float_as_uint(v), false, false); return __uint_as_float(s[0]) + __uint_as_float(s[1]); }
DEV float xsum32(float v) { auto s = __builtin_amdgcn_permlane32_swap(__float_as_uint(v), __float_as_uint(v), false, false); return __uint_as_float(s[0]) + __uint_as_float(s[1]); }
DEV float xsum8(float v) { return v + __uint_as_float(__builtin_amdgcn_update_dpp(0u, __float_as_uint(v), 0x128, 0xf, 0xf, true)); }

#define ABL_NO_A 0
#define ABL_NO_MFMA 0
#define ABL_NO_LDS 0
#define ABL_NO_DMA 0
#define STAMPS 0
constexpr unsigned STAMP_OFF = 96u << 20;
#define PROBE_SLOT -1
#define PROBE_I 0
#define PROBE_W 0
#define PROBE_WG 0
#define PROBE_K 8
constexpr int PROBE_LDS = 160 * 1024 - 16;
#define TS_DECL do {} while (0)
#define TS(i) do {} while (0)
#define TS_OUT(p, slot, b, w, lane) do {} while (0)
DEV gu32* ctl(const Params& p, int line) { return (gu32*)p.ws + line * 32; }
DEV void poll_ge(const Params& p, int line, unsigned want) {
  gu32* w = ctl(p, line);
  for (unsigned spins = 0;; ++spins) {
    if (__builtin_amdgcn_readfirstlane(__hip_atomic_load(w, RLX_AGENT)) >= want) break;
    if (spins > SPIN_CAP) { __hip_atomic_store(ctl(p, C_TMO), 1u, RLX_AGENT); break; }
  }
  __builtin_amdgcn_fence(__ATOMIC_ACQUIRE, "wavefront");
}
DEV void poll_shards(const Params& p, int line0, int lane) {
  gu32* base = ctl(p, line0);
  for (unsigned spins = 0;; ++spins) {
    const unsigned v = lane < 16 ? __hip_atomic_load(base + lane * 32, RLX_AGENT) : 16u;
    if (__all(v >= 16u)) break;
    if (spins > SPIN_CAP) { __hip_atomic_store(ctl(p, C_TMO), 1u, RLX_AGENT); break; }
  }
  __builtin_amdgcn_fence(__ATOMIC_ACQUIRE, "wavefront");
}
DEV void signal(const Params& p, int line) { __hip_atomic_fetch_add(ctl(p, line), 1u, RLX_AGENT); }

template <int I> struct IC { static constexpr int v = I; constexpr operator int() const { return I; } };
template <int... I, class F> DEV void sfor_impl(std::integer_sequence<int, I...>, F&& f) { int dummy[] = {0, (f(IC<I>{}), 0)...}; (void)dummy; }
template <int N, class F> DEV void sfor(F&& f) { sfor_impl(std::make_integer_sequence<int, N>{}, f); }
template <int N, int P, class RD, class PRE, class MM> DEV void pipeline(RD rd, PRE pre, MM mm) {
  v4u q[P];
  sfor<P>([&](auto i) { q[i] = rd(i); });
  __builtin_amdgcn_sched_barrier(0);
  sfor<N>([&](auto n) {
    const v4u cur = q[n % P];
    pre(n);
    if constexpr (n + P < N) q[n % P] = rd(IC<n + P>{});
    mm(n, cur);
    __builtin_amdgcn_sched_barrier(0);
  });
}
struct LBase { LAS char* b[3]; };
DEV LBase lbase(LAS char* p) {
  LBase r; unsigned u = (unsigned)(size_t)p;
  unsigned u0 = u, u1 = u + 65536u, u2 = u + 131072u;
  asm volatile("" : "+v"(u0), "+v"(u1), "+v"(u2));
  r.b[0] = (LAS char*)(size_t)u0; r.b[1] = (LAS char*)(size_t)u1; r.b[2] = (LAS char*)(size_t)u2; return r;
}
template <int OFF> DEV v4u lrd(const LBase& r) { return *(LAS v4u*)(r.b[OFF >> 16] + (OFF & 65535)); }
DEV int opq_v(int x) { asm volatile("" : "+v"(x)); return x; }
DEV int opq_s(int x) { asm volatile("" : "+s"(x)); return x; }

constexpr int S_NSLOT = 18;
#define SWIN 8
#define SP 8
#define WTHR 8
constexpr int S_WIN = SWIN, S_P = SP;
template <int I> struct SWait { static constexpr int issued = (9 + (I > 2 ? I - 2 : 0)) > 16 ? 16 : (9 + (I > 2 ? I - 2 : 0));
  static constexpr int n0 = 16 * (issued - (I + 1)); static constexpr int n = n0 > 63 ? 63 : n0; };
struct SGeo { rsrc_t rre, rim; int v0, v1, sbase, rot; };
DEV void s_map(int b, int& tb, int& j, int& rot) { const int x = b & 7, s = b >> 3; tb = 8 * x + (s >> 2); j = s & 3; rot = (4 * j + (tb & 3)) & 15; }
DEV SGeo s_geo(const Params& p, int b, int lane) {
  b = opq_s(b); lane = opq_v(lane);
  int tb, j; SGeo g; s_map(b, tb, j, g.rot);
  g.rre = mk(p.cre, 2048u * 4096u); g.rim = mk(p.cim, 2048u * 4096u);
  g.v0 = (lane >> 3) * 4096 + (((lane & 7) ^ (lane >> 4)) << 4);
  g.v1 = (8 + (lane >> 3)) * 4096 + (((lane & 7) ^ (4 + (lane >> 4))) << 4);
  g.sbase = tb * 32 * 4096;
  return g;
}
template <int LO, int HI> DEV void s_issue(const SGeo& g, LAS char* lds) {
  sfor<HI - LO>([&](auto i) {
    constexpr int seq = LO + i;
    const int so = g.sbase + ((seq + 2 * g.rot) & 31) * 128;
    sfor<8>([&](auto pc) { dma16<0>(((pc >> 1) & 1) ? g.rim : g.rre, lds + (seq % S_NSLOT) * 8192 + pc * 1024, (pc & 1) ? g.v1 : g.v0, so + (pc >> 2) * 65536); });
  });
}
template <int I> DEV void s_loader_step(const Params& p, const SGeo& g, LAS char* lds, int layer, int b, int lane) {
  waitv<SWait<I>::n>();
  bar();
  if constexpr (I == 0) {
    if (layer == 0 && lane < 8) signal(p, C_W + lane * 16 + (b >> 4));
  }
  if constexpr (I >= 2 && I + 7 < 16) s_issue<2 * (I + 7), 2 * (I + 7) + 2>(g, lds);
  if constexpr (I == 12) { if (layer == 0) poll_shards(p, C_W + (b & 7) * 16, lane); }
  if constexpr (I < 15) s_loader_step<I + 1>(p, g, lds, layer, b, lane);
}
DEV void S_load(const Params& p, LAS char* lds, rsrc_t rws, int layer, int b, int lane, unsigned long long tag) {
  const SGeo g = s_geo(p, b, lane);
  int tb, j, rot; s_map(b, tb, j, rot);
  TS_DECL; TS(0);
  s_issue<7, 12>(g, lds);
  TS(1);
  if (layer == 0) {
    for (unsigned spins = 0;; ++spins) {
      asm volatile("" ::: "memory");
      const v4u t0 = ld16<AUX_SC1>(rws, lane * 32, TAG_OFF + (b & 7) * 2048), t1 = ld16<AUX_SC1>(rws, lane * 32 + 16, TAG_OFF + (b & 7) * 2048);
      const unsigned lo = (unsigned)tag, hi = (unsigned)(tag >> 32);
      const bool ok = t0.x == lo && t0.y == hi && t0.z == lo && t0.w == hi && t1.x == lo && t1.y == hi && t1.z == lo && t1.w == hi;
      if (__all(ok)) break;
      if (spins > SPIN_CAP) { __hip_atomic_store(ctl(p, C_TMO), 1u, RLX_AGENT); break; }
    }
    __builtin_amdgcn_fence(__ATOMIC_ACQUIRE, "wavefront");
    bar();
  } else {
    poll_shards(p, C_X + (b & 7) * 16, lane);
    if (b == 0 && lane == 0) __hip_atomic_store((GAS unsigned long long*)(p.ws + EPOCH_OFF), tag, RLX_AGENT);
  }
  TS(2);
  s_issue<12, 18>(g, lds);
  s_loader_step<0>(p, g, lds, layer, b, lane);
  TS(3);
  bar();
  if (lane == 0) signal(p, C_S + layer * 64 + tb);
  TS(5); TS_OUT(p, 1 + layer * 5, b, 7, lane);
}
template <int LO, int HI, bool THROTTLE, class IDX> DEV void wload(rsrc_t rws, int base, int lane, v4u (&a)[28], IDX idx) {
  sfor<HI - LO>([&](auto k) { constexpr int i = idx(LO + k); a[i] = ld16<HAUX>(rws, lane * 16, base + i * 1024); if constexpr (THROTTLE) waitv<WTHR>(); });
  __builtin_amdgcn_sched_barrier(0);
}
constexpr int ord_h(int i) { return i; }
constexpr int ord_g(int i) { return (i >> 1) + 14 * (i & 1); }
DEV int h_wbase(int b, int w) { return WD_OFF + (7 * ((b >> 3) & 3) + w) * 28 * 1024; }
DEV void S_cons(const Params& p, LAS char* lds, rsrc_t rws, int layer, int b, int w, int lane, v4u (&an)[28]) {
  b = opq_s(b); lane = opq_v(lane);
  int tb, j, rot; s_map(b, tb, j, rot);
  const int lay = LAY_OFF + layer * LAY_BYTES;
  TS_DECL; TS(0);
  bar();
  TS(1);
  const int df = 7 * j + w;
  const int abase = lay + XN_O + df * 64 * 1024;
  v4u a[64];
  sfor<S_WIN>([&](auto i) { a[i] = ld16<HAUX>(rws, lane * 16, abase + ((i + 4 * rot) & 63) * 1024); });
  f32x4 acc[4] = {};
  const int t = lane & 15, kg = lane >> 4;
  const LBase b0 = lbase(lds + t * 128 + ((kg ^ (t >> 1)) << 4));
  const LBase b1 = lbase(lds + t * 128 + (((4 + kg) ^ (t >> 1)) << 4));
  pipeline<256, S_P>(
      [&](auto n) { constexpr int ks = n >> 2; return lrd<((ks >> 1) % S_NSLOT) * 8192 + (n & 3) * 2048>((ks & 1) ? b1 : b0); },
      [&](auto n) {
        if constexpr (((n + S_P) & 15) == 0 && n + S_P < 256) bar();
        if constexpr ((n & 3) == 0 && S_WIN + (n >> 2) < 64) a[S_WIN + (n >> 2)] = ld16<HAUX>(rws, lane * 16, abase + ((S_WIN + (n >> 2) + 4 * rot) & 63) * 1024);
      },
      [&](auto n, v4u bf) { acc[n & 3] = mfma(a[n >> 2], bf, acc[n & 3]); });
  TS(3);
  const int row = lane >> 4, kind = row & 1, half = row >> 1;
#pragma unroll
  for (int tg = 0; tg < 2; ++tg) {
    const f32x4 r = acc[2 * tg], m = acc[2 * tg + 1];
    const v4u c = pair_chunk(pack2(r[0], r[1]), pack2(r[2], r[3]), pack2(m[0], m[1]), pack2(m[2], m[3]));
    const int tt = 2 * tb + tg;
    st16<HAUX>(c, rws, kind * 14 * 1024 + ((2 * (df & 1) + half) * 16 + t) * 16, lay + PQ_O + (tt * 28 + (df >> 1)) * 1024);
  }
  __builtin_amdgcn_sched_barrier(0);
  const int wb = h_wbase(b, w);
  wload<0, WTHR, false>(rws, wb, lane, an, [](int i) { return ord_h(i); });
  waitv<WTHR>();
  TS(4);
  bar();
  wload<WTHR, 16, true>(rws, wb, lane, an, [](int i) { return ord_h(i); });
  TS(5); TS_OUT(p, 1 + layer * 5, b, w, lane);
}

DEV void g_map(int b, int& c, int& ft) { const int x = b & 7, s = b >> 3; c = 8 * (x & 1) + (s >> 2); ft = 4 * (x >> 1) + (s & 3); }
DEV int g_wbase(int b, int w) { int c, ft; g_map(b, c, ft); return WGU_OFF + (ft * 7 + w) * 28 * 1024; }
DEV void H_load(const Params& p, LAS char* lds, rsrc_t rws, int layer, int b, int lane) {
  b = opq_s(b); lane = opq_v(lane);
  const int x = b & 7, s = b >> 3, tb = 8 * x + (s >> 2), j = s & 3;
  const int lay = LAY_OFF + layer * LAY_BYTES;
  LAS float* red = (LAS float*)(lds + SCR);
  TS_DECL; TS(0);
  poll_ge(p, C_S + layer * 64 + tb, 4u);
  TS(1);
  const int src = lay + PQ_O + 2 * tb * 28 * 1024;
#pragma unroll
  for (int ks = 0; ks < 28; ++ks) {
    dma16<HAUX>(rws, lds + (ks * 2) * 1024, lane * 16, src + ks * 1024);
    dma16<HAUX>(rws, lds + (ks * 2 + 1) * 1024, lane * 16, src + (28 + ks) * 1024);
    if (ks == 15) { waitv<24>(); bar(); }
  }
  TS(2);
  waitv<32>(); bar();
  waitv<0>(); TS(3); bar();
  bar();
  if (lane < 32) {
    float mean = 0.f;
#pragma unroll
    for (int ww = 0; ww < 7; ++ww) mean += red[(ww * 32 + lane) * 2];
    mean *= (1.f / 7.f);
    float m2 = 0.f;
#pragma unroll
    for (int ww = 0; ww < 7; ++ww) { const float d = red[(ww * 32 + lane) * 2] - mean; m2 += red[(ww * 32 + lane) * 2 + 1] + 16.f * d * d; }
    const int c = tb >> 2, tok = (tb & 3) * 32 + lane;
    __builtin_amdgcn_raw_buffer_store_b64(v2u{__float_as_uint(mean), __float_as_uint(m2)}, rws, lay + ST_O + ((c * 4 + j) * 128 + tok) * 8, 0, HAUX);
  }
  WAITV(0);
  bar();
  if (lane == 0) signal(p, C_H + layer * 16 + (tb >> 2));
  TS(5); TS_OUT(p, 2 + layer * 5, b, 7, lane);
}
DEV void H_cons(const Params& p, LAS char* lds, rsrc_t rws, int layer, int b, int w, int lane, v4u (&a)[28], v4u (&an)[28]) {
  b = opq_s(b); lane = opq_v(lane);
  const int x = b & 7, s = b >> 3, tb = 8 * x + (s >> 2), j = s & 3;
  const int lay = LAY_OFF + layer * LAY_BYTES;
  LAS float* red = (LAS float*)(lds + SCR);
  TS_DECL; TS(0);
  const int ef = 7 * j + w;
  const int row = lane >> 4, t2 = row & 1, half = row >> 1, tok = lane & 15;
  const int kg = 2 * (ef & 1) + half;
  f32x4 acc[2] = {};
  LAS char* bb = lds + lane * 16;
  const int wb = h_wbase(b, w);
  constexpr int P = 6;
  bar();
  TS(2);
  const v4u res = ld16<HAUX>(rws, t2 * 14 * 1024 + (kg * 16 + tok) * 16, lay + XR_O + (2 * tb * 14 + (ef >> 1)) * 1024);
  pipeline<56, P>([&](auto n) { return *(LAS v4u*)(bb + n * 1024); },
                  [&](auto n) { if constexpr (n + P == 8 || n + P == 24) bar();
                                if constexpr ((n & 1) == 0 && 16 + (n >> 1) < 28) a[16 + (n >> 1)] = ld16<HAUX>(rws, lane * 16, wb + (16 + (n >> 1)) * 1024); },
                  [&](auto n, v4u bf) { acc[n & 1] = mfma(a[n >> 1], bf, acc[n & 1]); });
  TS(3);
  float y[8];
#pragma unroll
  for (int i = 0; i < 4; ++i) {
    auto sw = __builtin_amdgcn_permlane16_swap(__float_as_uint(acc[0][i]), __float_as_uint(acc[1][i]), false, false);
    y[i] = __uint_as_float(sw[0]); y[4 + i] = __uint_as_float(sw[1]);
  }
  v4u o; float v[8];
#pragma unroll
  for (int i = 0; i < 4; ++i) {
    o[i] = pack2(blo(res[i]) + y[2 * i], bhi(res[i]) + y[2 * i + 1]);
    v[2 * i] = blo(o[i]); v[2 * i + 1] = bhi(o[i]);
  }
  const int c = tb >> 2, tp = tb & 3;
  st16<HAUX>(o, rws, (c * 4 + tp) * 28 * 1024 + t2 * 1024 + (kg * 16 + tok) * 16, lay + X1_O + (ef >> 1) * 2048);
  const float sm = ((v[0] + v[1]) + (v[2] + v[3])) + ((v[4] + v[5]) + (v[6] + v[7]));
  const float mean = xsum32(sm) * (1.f / 16.f);
  float m2 = 0.f;
#pragma unroll
  for (int i = 0; i < 8; ++i) { const float d = v[i] - mean; m2 += d * d; }
  m2 = xsum32(m2);
  if (half == 0) { red[(w * 32 + t2 * 16 + tok) * 2] = mean; red[(w * 32 + t2 * 16 + tok) * 2 + 1] = m2; }
  __builtin_amdgcn_sched_barrier(0);
  const int gb = g_wbase(b, w);
  wload<0, WTHR, false>(rws, gb, lane, an, [](int i) { return ord_g(i); });
  lbar();
  waitv<WTHR>();
  TS(4);
  bar();
  wload<WTHR, 20, true>(rws, gb, lane, an, [](int i) { return ord_g(i); });
  TS(5); TS_OUT(p, 2 + layer * 5, b, w, lane);
}

DEV void d_map(int b, int& c, int& j, int& q) { const int x = b & 7, s = b >> 3; c = 8 * (x & 1) + (s >> 2); j = s & 3; q = x >> 1; }
DEV void load_tile112(rsrc_t rws, LAS char* lds, int src, int lane) {
#pragma unroll
  for (int i = 0; i < 56; ++i) dma16<HAUX>(rws, lds + i * 1024, lane * 16, src + i * 1024);
  waitv<28>(); bar();
#pragma unroll
  for (int i = 56; i < 112; ++i) dma16<HAUX>(rws, lds + i * 1024, lane * 16, src + i * 1024);
}
constexpr int CS_LDS = SCR + 1024;
DEV void G_load(const Params& p, LAS char* lds, rsrc_t rws, int layer, int b, int lane) {
  b = opq_s(b); lane = opq_v(lane);
  int c, ft; g_map(b, c, ft);
  const int lay = LAY_OFF + layer * LAY_BYTES;
  LAS float* mr = (LAS float*)(lds + SCR);
  TS_DECL; TS(0);
  if (lane < 56) {
    f32x4 cs = {0.f, 0.f, 0.f, 0.f};
    v4u pp[14];
#pragma unroll
    for (int ks = 0; ks < 14; ++ks) pp[ks] = ld16<HAUX>(rws, lane * 16, CSP_OFF + (ft * 14 + ks) * 896);
#pragma unroll
    for (int ks = 0; ks < 14; ++ks) cs += __builtin_bit_cast(f32x4, pp[ks]);
    *(LAS f32x4*)(lds + CS_LDS + lane * 16) = cs;
  }
  poll_ge(p, C_H + layer * 16 + c, 16u);
  TS(1);
  v2u st[2][4];
#pragma unroll
  for (int hh = 0; hh < 2; ++hh)
#pragma unroll
    for (int j = 0; j < 4; ++j) st[hh][j] = __builtin_amdgcn_raw_buffer_load_b64(rws, ((c * 4 + j) * 128 + hh * 64 + lane) * 8, lay + ST_O, HAUX);
  __builtin_amdgcn_sched_barrier(0);
  load_tile112(rws, lds, lay + X1_O + c * TILE112, lane);
  TS(2);
  waitv<56>(); bar();
  __builtin_amdgcn_sched_barrier(0);
#pragma unroll
  for (int hh = 0; hh < 2; ++hh) {
    float mean = 0.f;
#pragma unroll
    for (int j = 0; j < 4; ++j) mean += __uint_as_float(st[hh][j].x);
    mean *= 0.25f;
    float m2 = 0.f;
#pragma unroll
    for (int j = 0; j < 4; ++j) { const float d = __uint_as_float(st[hh][j].x) - mean; m2 += __uint_as_float(st[hh][j].y) + 112.f * d * d; }
    mr[(hh * 64 + lane) * 2] = mean;
    mr[(hh * 64 + lane) * 2 + 1] = __builtin_amdgcn_rsqf(m2 * (1.f / 448.f) + 1e-5f);
  }
  waitv<0>(); TS(3); lbar();
  bar();
  if (lane == 0) signal(p, C_G + (layer * 16 + c) * 4 + (ft >> 2));
  TS(5); TS_OUT(p, 3 + layer * 5, b, 7, lane);
}
DEV void G_cons(const Params& p, LAS char* lds, rsrc_t rws, int layer, int b, int w, int lane, v4u (&a)[28], v4u (&an)[28]) {
  b = opq_s(b); lane = opq_v(lane);
  int c, ft; g_map(b, c, ft);
  const int lay = LAY_OFF + layer * LAY_BYTES;
  LAS float* mr = (LAS float*)(lds + SCR);
  TS_DECL; TS(0);
  const int row = lane >> 4, t2l = row & 1, half = row >> 1, tok = lane & 15;
  const int ff = 7 * ft + w, ksg = ff >> 1, q = ksg / 14, ks14 = ksg - 14 * q;
  const int hdst = lay + HB_O + (c * 4 + q) * TILE112 + ks14 * 2048 + t2l * 1024 + ((2 * (ff & 1) + half) * 16 + tok) * 16;
  const int gb = g_wbase(b, w);
  int dc, dj, dq; d_map(b, dc, dj, dq);
  const int db = WDN_OFF + ((dq * 4 + dj) * 7 + w) * 14 * 1024;
  const LBase bb = lbase(lds + lane * 16);
  f32x4 ag[2][2], au[2][2], cg, cu;
  float h[8], mean[2], rs[2];
  auto elem = [&](int tp, int e) {
    const int t2 = e >> 2, i = e & 3;
    const float gg = rs[t2] * (ag[tp & 1][t2][i] - mean[t2] * cg[i]), uu = rs[t2] * (au[tp & 1][t2][i] - mean[t2] * cu[i]);
    h[e] = gg * uu * __builtin_amdgcn_rcpf(1.f + __expf(-gg));
    asm volatile("" : "+v"(h[e]));
  };
  auto stats = [&](int tp) {
#pragma unroll
    for (int t2 = 0; t2 < 2; ++t2) { mean[t2] = mr[((2 * tp + t2) * 16 + tok) * 2]; rs[t2] = mr[((2 * tp + t2) * 16 + tok) * 2 + 1]; }
  };
  auto put = [&](int tp) { st16<HAUX>(pair_chunk(pack2(h[0], h[1]), pack2(h[2], h[3]), pack2(h[4], h[5]), pack2(h[6], h[7])), rws, hdst + tp * 28 * 1024, 0); };
  constexpr int P = 4, CSTEP = 30;
  __builtin_amdgcn_sched_barrier(0);
  TS(1);
  bar();
  TS(2);
  pipeline<112, P>([&](auto n) { return lrd<n * 1024>(bb); },
                   [&](auto n) {
                     if constexpr (n + P == 28 || n == CSTEP) bar();
                     if constexpr (n < 8) a[ord_g(20 + n)] = ld16<HAUX>(rws, lane * 16, gb + ord_g(20 + n) * 1024);
                     if constexpr (n >= 40 && n < 68 && (n & 1) == 0) an[(n - 40) / 2] = ld16<HAUX>(rws, lane * 16, db + ((n - 40) / 2) * 1024);
                   },
                   [&](auto n, v4u bf) {
                     constexpr int tp = n / 28, r = n % 28, t2 = r & 1, ks = r >> 1;
                     if constexpr (ks == 0) { ag[tp & 1][t2] = mfma(a[0], bf, f32x4{0.f, 0.f, 0.f, 0.f}); au[tp & 1][t2] = mfma(a[14], bf, f32x4{0.f, 0.f, 0.f, 0.f}); }
                     else { ag[tp & 1][t2] = mfma(a[ks], bf, ag[tp & 1][t2]); au[tp & 1][t2] = mfma(a[14 + ks], bf, au[tp & 1][t2]); }
                     if constexpr (tp > 0) {
                       if constexpr (r == 2) { stats(tp - 1);
                         if constexpr (tp == 1) { cg = *(LAS f32x4*)(lds + CS_LDS + (16 * w + 4 * row) * 4); cu = *(LAS f32x4*)(lds + CS_LDS + (112 + 16 * w + 4 * row) * 4); } }
                       if constexpr (r >= 6 && r <= 20 && (r & 1) == 0) elem(tp - 1, (r - 6) / 2);
                       if constexpr (r == 23) put(tp - 1);
                     }
                   });
  TS(3);
  stats(3);
#pragma unroll
  for (int e = 0; e < 8; ++e) elem(3, e);
  put(3);
  WAITV(0);
  TS(4);
  bar();
  TS(5); TS_OUT(p, 3 + layer * 5, b, w, lane);
}

DEV void D_load(const Params& p, LAS char* lds, rsrc_t rws, int layer, int b, int lane) {
  b = opq_s(b); lane = opq_v(lane);
  int c, j, q; d_map(b, c, j, q);
  const int lay = LAY_OFF + layer * LAY_BYTES;
  TS_DECL; TS(0);
  poll_ge(p, C_G + (layer * 16 + c) * 4 + q, 4u);
  TS(1);
  load_tile112(rws, lds, lay + HB_O + (c * 4 + q) * TILE112, lane);
  TS(2);
  waitv<56>(); bar();
  waitv<0>(); TS(3); bar();
  bar();
  if (lane == 0) signal(p, C_D + layer * 16 + c);
  TS(5); TS_OUT(p, 4 + layer * 5, b, 7, lane);
}
DEV void D_cons(const Params& p, LAS char* lds, rsrc_t rws, int layer, int b, int w, int lane, v4u (&a)[28]) {
  b = opq_s(b); lane = opq_v(lane);
  int c, j, q; d_map(b, c, j, q);
  const int lay = LAY_OFF + layer * LAY_BYTES;
  TS_DECL; TS(0);
  const int row = lane >> 4, t2l = row & 1, half = row >> 1, tok = lane & 15;
  const int df = 7 * j + w;
  const int ddst = lay + DP_O + (q * 16 + c) * TILE112 + (df >> 1) * 2048 + t2l * 1024 + ((2 * (df & 1) + half) * 16 + tok) * 16;
  const LBase bb = lbase(lds + lane * 16);
  f32x4 y[2][2];
  auto put = [&](int tp) { const f32x4 y0 = y[tp & 1][0], y1 = y[tp & 1][1];
    st16<HAUX>(pair_chunk(pack2(y0[0], y0[1]), pack2(y0[2], y0[3]), pack2(y1[0], y1[1]), pack2(y1[2], y1[3])), rws, ddst + tp * 28 * 1024, 0); };
  constexpr int P = 8;
  TS(1);
  bar();
  TS(2);
  pipeline<112, P>([&](auto n) { return lrd<n * 1024>(bb); },
                   [&](auto n) { if constexpr (n + P == 28 || n + P == 56) bar(); },
                   [&](auto n, v4u bf) {
                     constexpr int tp = n / 28, r = n % 28, t2 = r & 1, ks = r >> 1;
                     y[tp & 1][t2] = mfma(a[ks], bf, ks == 0 ? f32x4{0.f, 0.f, 0.f, 0.f} : y[tp & 1][t2]);
                     if constexpr (tp > 0 && r == 4) put(tp - 1);
                   });
  TS(3);
  put(3);
  WAITV(0);
  TS(4);
  bar();
  TS(5); TS_OUT(p, 4 + layer * 5, b, w, lane);
}

DEV void ln_publish(rsrc_t rws, LAS char* lds, int laynext, int tok0  , int tid, const float (&v)[8]) {
  LAS float* red = (LAS float*)(lds + SCR);
  LAS char* tr = lds + SCR + 1024;
  const int wv = tid >> 6, tok = tid & 7, c56 = tid >> 3;
  float mean = 0.f, rs = 0.f;
  if (tid < 448) {
    float sm = ((v[0] + v[1]) + (v[2] + v[3])) + ((v[4] + v[5]) + (v[6] + v[7]));
    sm = xsum32(xsum16(xsum8(sm)));
    const float mw = sm * (1.f / 64.f);
    float m2 = 0.f;
#pragma unroll
    for (int i = 0; i < 8; ++i) { const float d = v[i] - mw; m2 += d * d; }
    m2 = xsum32(xsum16(xsum8(m2)));
    if ((tid & 63) < 8) { red[(wv * 8 + tok) * 2] = mw; red[(wv * 8 + tok) * 2 + 1] = m2; }
  }
  lbar();
  if (tid < 448) {
#pragma unroll
    for (int ww = 0; ww < 7; ++ww) mean += red[(ww * 8 + tok) * 2];
    mean *= (1.f / 7.f);
    float m2 = 0.f;
#pragma unroll
    for (int ww = 0; ww < 7; ++ww) { const float d = red[(ww * 8 + tok) * 2] - mean; m2 += red[(ww * 8 + tok) * 2 + 1] + 64.f * d * d; }
    rs = __builtin_amdgcn_rsqf(m2 * (1.f / 448.f) + 1e-5f);
#pragma unroll
    for (int i = 0; i < 8; ++i) {
      const __bf16 hv = (__bf16)((v[i] - mean) * rs);
      *(LAS unsigned short*)(tr + (c56 * 8 + i) * 16 + c56 * 16 + tok * 2) = __builtin_bit_cast(unsigned short, hv);
    }
  }
  lbar();
  if (tid < 448) {
    const int d = tid;
    const v4u o = *(LAS v4u*)(tr + d * 16 + (d >> 3) * 16);
    st16<HAUX>(o, rws, ((d >> 4) * 64) * 1024 + (d & 15) * 16, laynext + XN_O + (tok0 >> 5) * 1024 + ((tok0 >> 3) & 3) * 256);
  }
}
DEV void phase_X(const Params& p, LAS char* lds, rsrc_t rws, int layer, int b, int w, int lane) {
  b = opq_s(b); lane = opq_v(lane);
  const int x = b & 7, s = b >> 3, c = 2 * x + (s >> 4), r = s & 15;
  const int lay = LAY_OFF + layer * LAY_BYTES;
  const int tid = w * 64 + lane;
  TS_DECL; TS(0);
  if (w == 0) poll_ge(p, C_D + layer * 16 + c, 16u);
  bar();
  TS(1);
  float v[8];
  const int tok = tid & 7, c56 = tid >> 3, ks = c56 >> 2, kg = c56 & 3;
  if (tid < 448) {
    const int tp = r >> 2, t2 = (r >> 1) & 1, tok16 = 8 * (r & 1) + tok;
    const int off = (tp * 14 + ks) * 2048 + t2 * 1024 + (kg * 16 + tok16) * 16;
    v4u pr[4];
#pragma unroll
    for (int q = 0; q < 4; ++q) pr[q] = ld16<HAUX>(rws, off, lay + DP_O + (q * 16 + c) * TILE112);
    const v4u x1 = ld16<HAUX>(rws, off, lay + X1_O + c * TILE112);
    v4u o;
#pragma unroll
    for (int i = 0; i < 4; ++i) {
      const float lo = blo(x1[i]) + ((blo(pr[0][i]) + blo(pr[1][i])) + (blo(pr[2][i]) + blo(pr[3][i])));
      const float hi = bhi(x1[i]) + ((bhi(pr[0][i]) + bhi(pr[1][i])) + (bhi(pr[2][i]) + bhi(pr[3][i])));
      o[i] = pack2(lo, hi); v[2 * i] = blo(o[i]); v[2 * i + 1] = bhi(o[i]);
    }
    if (layer == 1) st16<0>(o, mk(p.out, 2048u * 896u), (128 * c + 8 * r + tok) * 896 + c56 * 16, 0);
    else st16<HAUX>(o, rws, ((8 * c + (r >> 1)) * 14 + ks) * 1024 + (kg * 16 + tok16) * 16, lay + LAY_BYTES + XR_O);
  }
  if (layer == 0) {
    ln_publish(rws, lds, lay + LAY_BYTES, 128 * c + 8 * r, tid, v);
    WAITV(0);
    bar();
    if (tid >= 448 && tid < 456) signal(p, C_X + (tid - 448) * 16 + c);
  }
  TS(5); TS_OUT(p, 5 + layer * 5, b, w, lane);
}

DEV void P_cons(const Params& p, LAS char* lds, rsrc_t rws, int b, int w, int lane) {
  const int tid = w * 64 + lane;
  TS_DECL; TS(0);
  const int tok = tid & 7, c56 = tid >> 3;
  const v4u xv = ld16<0>(mk(p.x, 2048u * 896u), (8 * b + tok) * 896 + c56 * 16, 0);
  const int job = w * 256 + b;
  v4u src[7];
  int dst = 0, dstr = 0, csp = -1;
  if (job < 672) {
    const char* sp; unsigned bytes; int ld, goff;
    if (job < 448) { const int ft = job / 28, rem = job - ft * 28, gu = rem / 14, ks = rem - gu * 14;
      sp = gu ? p.wu : p.wg; bytes = 448u * 3584u; ld = 3584; goff = ks * 32 * ld + ft * 224;
      dst = WGU_OFF + ((ft * 14 + gu) * 14 + ks) * 1024; dstr = 28 * 1024; csp = CSP_OFF + (ft * 14 + ks) * 896 + gu * 448;
    } else { const int jj = job - 448, j = jj / 56, ksg = jj - j * 56, q = ksg / 14, ks = ksg - q * 14;
      sp = p.wdn; bytes = 1792u * 896u; ld = 896; goff = ksg * 32 * ld + j * 224;
      dst = WDN_OFF + (((q * 4 + j) * 7) * 14 + ks) * 1024; dstr = 14 * 1024; }
    const rsrc_t rsrc = mk(sp, bytes);
#pragma unroll
    for (int i = 0; i < 7; ++i) { const int ch = i * 64 + lane, r = ch / 14, cc = ch - r * 14; src[i] = ld16<0>(rsrc, goff + r * ld + cc * 16, 0); }
  } else if (job < 672 + 784) {
    const int blk = job - 672, ef = blk / 28, ks = blk - ef * 28;
    const bool im = ks >= 14;
    src[0] = ld16<0>(mk(im ? p.wdim : p.wdre, 448u * 896u), (16 * ef + (lane & 15)) * 896 + (im ? ks - 14 : ks) * 64 + (lane >> 4) * 16, 0);
    if (im) src[0] ^= 0x80008000u;
    dst = WD_OFF + blk * 1024;
  }
  if (w == 0 && lane < 3) { const int line = b + 256 * lane; if (line < C_LINES) __hip_atomic_store(ctl(p, line), 0u, RLX_AGENT); }
  float v[8];
#pragma unroll
  for (int i = 0; i < 4; ++i) { v[2 * i] = blo(xv[i]); v[2 * i + 1] = bhi(xv[i]); }
  st16<HAUX>(xv, rws, ((b >> 1) * 14 + (c56 >> 2)) * 1024 + ((c56 & 3) * 16 + 8 * (b & 1) + tok) * 16, LAY_OFF + XR_O);
  TS(1);
  ln_publish(rws, lds, LAY_OFF, 8 * b, tid, v);
  WAITV(0);
  TS(2);
  bar();
  if (job < 672) {
    LAS char* t = lds + 120 * 1024 + w * 8192;
#pragma unroll
    for (int i = 0; i < 7; ++i) { const int ch = i * 64 + lane, r = ch / 14, cc = ch - r * 14; *(LAS v4u*)(t + r * 240 + cc * 16) = src[i]; }
    WAITL0();
    const int n = lane & 15, kg = lane >> 4;
#pragma unroll
    for (int f = 0; f < 7; ++f) {
      unsigned e[8];
#pragma unroll
      for (int i = 0; i < 8; ++i) e[i] = *(LAS unsigned short*)(t + (8 * kg + i) * 240 + (16 * f + n) * 2);
      st16<HAUX>(v4u{e[0] | (e[1] << 16), e[2] | (e[3] << 16), e[4] | (e[5] << 16), e[6] | (e[7] << 16)}, rws, lane * 16, dst + f * dstr);
      if (csp >= 0) {
        float sm = 0.f;
#pragma unroll
        for (int i = 0; i < 8; ++i) sm += blo(e[i]);
        sm = xsum32(xsum16(sm));
        if (lane < 16) __builtin_amdgcn_raw_buffer_store_b32(__float_as_uint(sm), rws, csp + (16 * f + n) * 4, 0, HAUX);
      }
    }
  } else if (job < 672 + 784) st16<HAUX>(src[0], rws, lane * 16, dst);
  WAITV(0);
  WAITL0();
  TS(4);
  bar();
  TS(5); TS_OUT(p, 0, b, w, lane);
}
DEV unsigned long long P_load(const Params& p, LAS char* lds, rsrc_t rws, int b, int lane) {
  TS_DECL; TS(0);
  const unsigned long long tag = __hip_atomic_load((GAS unsigned long long*)(p.ws + EPOCH_OFF), RLX_AGENT) + 1ull;
  { const SGeo g = s_geo(p, b, lane); s_issue<0, 7>(g, lds); }
  TS(1);
  lbar(); lbar();
  bar();
  if (lane < 8) __hip_atomic_store((GAS unsigned long long*)(p.ws + TAG_OFF) + lane * 256 + b, tag, RLX_AGENT);
  TS(5); TS_OUT(p, 0, b, 7, lane);
  return tag;
}

__global__ void __launch_bounds__(NTHR, 2) k_main(Params p) {
  extern __shared__ __attribute__((aligned(16))) char smem[];
  LAS char* lds = (LAS char*)smem;
  const rsrc_t rws = mk(p.ws, WS_BYTES);
  const int lane = threadIdx.x & 63, w = __builtin_amdgcn_readfirstlane(threadIdx.x >> 6), b = blockIdx.x;
  if (w == 7) {
    __builtin_amdgcn_s_setprio(3);
    const unsigned long long tag = P_load(p, lds, rws, b, lane);
#pragma nounroll
    for (int layer = 0; layer < 2; ++layer) {
      S_load(p, lds, rws, layer, b, lane, tag);
      H_load(p, lds, rws, layer, b, lane);
      G_load(p, lds, rws, layer, b, lane);
      D_load(p, lds, rws, layer, b, lane);
      if (layer == 0) { const SGeo g = s_geo(p, b, lane); s_issue<0, 7>(g, lds); }
      phase_X(p, lds, rws, layer, b, 7, lane);
    }
  } else {
    P_cons(p, lds, rws, b, w, lane);
#pragma nounroll
    for (int layer = 0; layer < 2; ++layer) {
      v4u ah[28], ag[28], ad[28];
      S_cons(p, lds, rws, layer, b, w, lane, ah);
      H_cons(p, lds, rws, layer, b, w, lane, ah, ag);
      G_cons(p, lds, rws, layer, b, w, lane, ag, ad);
      D_cons(p, lds, rws, layer, b, w, lane, ad);
      phase_X(p, lds, rws, layer, b, w, lane);
    }
  }
}
}

extern "C" void kernel_launch(void* const* d_in, const int* in_sizes, int n_in, void* d_out, int out_size, void* d_ws, size_t ws_size,
                              hipStream_t stream) {
  using namespace fn;
  Params p;
  p.x = (const char*)d_in[0]; p.cre = (const char*)d_in[1]; p.cim = (const char*)d_in[2]; p.wdre = (const char*)d_in[3]; p.wdim = (const char*)d_in[4];
  p.wg = (const char*)d_in[6]; p.wu = (const char*)d_in[7]; p.wdn = (const char*)d_in[8]; p.out = (char*)d_out; p.ws = (char*)d_ws;
  (void)hipFuncSetAttribute((const void*)k_main, hipFuncAttributeMaxDynamicSharedMemorySize, LDS_BYTES);
  k_main<<<256, NTHR, LDS_BYTES, stream>>>(p);
}
```
